```python
import torch

def _softmax(x, axis=-1):
    return torch.softmax(x, dim=axis)
def _gelu(x):
    return torch.nn.functional.gelu(x, approximate="tanh")
def _silu(x):
    return torch.nn.functional.silu(x)
def _sigmoid(x):
    return torch.sigmoid(x)
def _layernorm(x, axis=-1, eps=1e-5):
    m = x.mean(dim=axis, keepdim=True)
    v = x.var(dim=axis, keepdim=True, unbiased=False)
    return (x - m) / torch.sqrt(v + eps)
def _scatter_add(tgt, idx, src, axis=0):
    return tgt.clone().index_add_(axis, idx, src)
def _segment_sum(src, seg, n):
    out = torch.zeros((n,) + src.shape[1:], dtype=src.dtype)
    return out.index_add_(0, seg, src)
def _pad(x, pw):
    flat = tuple(v for pair in reversed(pw) for v in pair)
    return torch.nn.functional.pad(x, flat)

kernel_name = 'sequential_preLN__mha_block_cached__glu_gated_bilinear__decode'

def setup_inputs():

    _w = (lambda a: (lambda t: t.to(torch.bfloat16) if t.is_floating_point() else t)(torch.as_tensor(a)))
    def _A_setup():
        B, Sq, D = 2, 1, 448
        H, Sk, Dh = 16, 8192, 256
        return {
            "x": _w((lambda *s: torch.randn(*s, dtype=torch.bfloat16) * 0.05)(B, Sq, D)),
            "k_cache": _w((lambda *s: torch.randn(*s, dtype=torch.bfloat16) * 1.6)(B, H, Sk, Dh)),
            "v_cache": _w((lambda *s: torch.randn(*s, dtype=torch.bfloat16) * 0.8)(B, H, Sk, Dh)),
            "wq": _w((lambda *s: torch.randn(*s, dtype=torch.bfloat16) * 1.6)(D, H * Dh) * (D ** -0.5)),
            "wo": _w((lambda *s: torch.randn(*s, dtype=torch.bfloat16) * 0.8)(H * Dh, D) * ((H * Dh) ** -0.5)),
        }
    _A_in = _A_setup()

    def _B_setup():
        B, S, D, F = 2, 1, 448, 1792
        return {
            "x": _w((lambda *s: torch.randn(*s, dtype=torch.bfloat16) * 0.05)(B, S, D)),
            "w_gate": _w((lambda *s: torch.randn(*s, dtype=torch.bfloat16) * 0.4)(D, F) * (D ** -0.5)),
            "w_up": _w((lambda *s: torch.randn(*s, dtype=torch.bfloat16) * 0.4)(D, F) * (D ** -0.5)),
            "w_down": _w((lambda *s: torch.randn(*s, dtype=torch.bfloat16) * 0.4)(F, D) * (F ** -0.5)),
        }
    _B_in = _B_setup()

    return {
        "A_x": _A_in["x"],
        "A_k_cache": _A_in["k_cache"],
        "A_v_cache": _A_in["v_cache"],
        "A_wq": _A_in["wq"],
        "A_wo": _A_in["wo"],
        "B_x": _B_in["x"],
        "B_w_gate": _B_in["w_gate"],
        "B_w_up": _B_in["w_up"],
        "B_w_down": _B_in["w_down"],
    }

def reference(A_x, A_k_cache, A_v_cache, A_wq, A_wo, B_x, B_w_gate, B_w_up, B_w_down):
    def _A_ref(x, k_cache, v_cache, wq, wo):
        B, Sq, D = x.shape
        H, Dh = 16, 256
        q = (lambda x, *d: x.permute(*d))((x @ wq).reshape(B, Sq, H, Dh), 0, 2, 1, 3)
        scores = torch.einsum("bhqd,bhkd->bhqk", q, k_cache) * (Dh ** -0.5)
        p = _softmax(scores, axis=-1)
        o = torch.einsum("bhqk,bhkd->bhqd", p, v_cache)
        return (lambda x, *d: x.permute(*d))(o, 0, 2, 1, 3).reshape(B, Sq, H * Dh) @ wo

    def _B_ref(x, w_gate, w_up, w_down):
        g = torch.einsum("bsd,df->bsf", x, w_gate)
        u = torch.einsum("bsd,df->bsf", x, w_up)
        h = g * u
        return torch.einsum("bsf,fd->bsd", h, w_down)

    x = A_x
    h = x + _A_ref(_layernorm(x, axis=-1), A_k_cache, A_v_cache, A_wq, A_wo)
    h2 = h + _B_ref(_layernorm(h, axis=-1), B_w_gate, B_w_up, B_w_down)
    return h2
```

```cpp
#include <hip/hip_runtime.h>
#include <hip/hip_bf16.h>
#include <stdint.h>

#define MIMIC 1

typedef unsigned short u16;
typedef unsigned u32x4 __attribute__((ext_vector_type(4)));
typedef __bf16 bf16x2 __attribute__((ext_vector_type(2)));

constexpr int B = 2, D = 448, H = 16, SK = 8192, DH = 256, F = 1792, HD = H * DH;
constexpr int NSPLIT = 16, KPS = SK / NSPLIT;

__device__ __forceinline__ float bf2f(u16 v) { return __builtin_bit_cast(float, (unsigned)v << 16); }
__device__ __forceinline__ float lo(unsigned v) { return __builtin_bit_cast(float, v << 16); }
__device__ __forceinline__ float hi(unsigned v) { return __builtin_bit_cast(float, v & 0xffff0000u); }
__device__ __forceinline__ u16 f2bf(float f) { __bf16 b = (__bf16)f; return __builtin_bit_cast(u16, b); }
__device__ __forceinline__ float rnd(float f) { return MIMIC ? bf2f(f2bf(f)) : f; }
__device__ __forceinline__ float wsum(float v) { for (int o = 32; o; o >>= 1) v += __shfl_xor(v, o); return v; }
__device__ __forceinline__ float wmax(float v) { for (int o = 32; o; o >>= 1) v = fmaxf(v, __shfl_xor(v, o)); return v; }

__device__ void layernorm_to_lds(const float* hrow, float* out, int lane) {
  float v[7]; float s = 0.f;
  for (int i = 0; i < 7; ++i) { v[i] = hrow[lane + 64 * i]; s += v[i]; }
  const float mean = wsum(s) * (1.f / D);
  float q = 0.f;
  for (int i = 0; i < 7; ++i) { const float d = v[i] - mean; q += d * d; }
  const float var = rnd(wsum(q) * (1.f / D));
  const float mb = rnd(mean);
  const float den = rnd(sqrtf(rnd(var + 1e-5f)));
  for (int i = 0; i < 7; ++i) out[lane + 64 * i] = rnd(rnd(v[i] - mb) / den);
}

__global__ void __launch_bounds__(256) k_q(const u16* x, const u16* wq, float* q) {
  __shared__ float xs[B][D], xn[B][D], red[4][B][64];
  const int tid = threadIdx.x, lane = tid & 63, w = tid >> 6;
  for (int i = tid; i < B * D; i += 256) xs[i / D][i % D] = bf2f(x[i]);
  __syncthreads();
  if (w < B) layernorm_to_lds(xs[w], xn[w], lane);
  __syncthreads();
  const int col = blockIdx.x * 64 + lane;
  float a0 = 0.f, a1 = 0.f;
  for (int k = w; k < D; k += 4) { const float wv = bf2f(wq[(size_t)k * HD + col]); a0 += xn[0][k] * wv; a1 += xn[1][k] * wv; }
  red[w][0][lane] = a0; red[w][1][lane] = a1;
  __syncthreads();
  if (w < B) q[w * HD + col] = rnd(red[0][w][lane] + red[1][w][lane] + red[2][w][lane] + red[3][w][lane]);
}

__global__ void __launch_bounds__(256) k_attn(const float* q, const u16* K, const u16* V, float* pm, float* pl, float* po) {
  __shared__ float sc[KPS], red[8][DH], wr[4];
  const int s = blockIdx.x % NSPLIT, bh = blockIdx.x / NSPLIT;
  const int tid = threadIdx.x, lane = tid & 63, w = tid >> 6, c = lane & 31, r = tid >> 5;
  float qf[8];
  for (int i = 0; i < 8; ++i) qf[i] = q[bh * DH + c * 8 + i];
  const u32x4* Kp = (const u32x4*)(K + ((size_t)bh * SK + (size_t)s * KPS) * DH);
  const u32x4* Vp = (const u32x4*)(V + ((size_t)bh * SK + (size_t)s * KPS) * DH);
  for (int it = 0; it < KPS / 64; ++it) {
    u32x4 kv[8];
#pragma unroll
    for (int j = 0; j < 8; ++j) kv[j] = __builtin_nontemporal_load(Kp + (size_t)(it * 64 + j * 8) * 32 + tid);
#pragma unroll
    for (int j = 0; j < 8; ++j) {
      float d = 0.f;
#pragma unroll
      for (int e = 0; e < 4; ++e) { d += lo(kv[j][e]) * qf[2 * e]; d += hi(kv[j][e]) * qf[2 * e + 1]; }
      for (int o = 16; o; o >>= 1) d += __shfl_xor(d, o);
      if (c == 0) sc[it * 64 + j * 8 + r] = rnd(d) * 0.0625f;
    }
  }
  __syncthreads();
  float m = -INFINITY;
  for (int i = tid; i < KPS; i += 256) m = fmaxf(m, sc[i]);
  m = wmax(m); if (lane == 0) wr[w] = m;
  __syncthreads();
  m = fmaxf(fmaxf(wr[0], wr[1]), fmaxf(wr[2], wr[3]));
  __syncthreads();
  float l = 0.f;
  for (int i = tid; i < KPS; i += 256) { const float p = __expf(sc[i] - m); sc[i] = p; l += p; }
  l = wsum(l); if (lane == 0) wr[w] = l;
  __syncthreads();
  l = wr[0] + wr[1] + wr[2] + wr[3];
  float acc[8] = {0, 0, 0, 0, 0, 0, 0, 0};
  for (int it = 0; it < KPS / 64; ++it) {
    u32x4 vv[8];
#pragma unroll
    for (int j = 0; j < 8; ++j) vv[j] = __builtin_nontemporal_load(Vp + (size_t)(it * 64 + j * 8) * 32 + tid);
#pragma unroll
    for (int j = 0; j < 8; ++j) {
      const float p = sc[it * 64 + j * 8 + r];
#pragma unroll
      for (int e = 0; e < 4; ++e) { acc[2 * e] += p * lo(vv[j][e]); acc[2 * e + 1] += p * hi(vv[j][e]); }
    }
  }
  for (int i = 0; i < 8; ++i) red[r][c * 8 + i] = acc[i];
  __syncthreads();
  float o = 0.f;
  for (int i = 0; i < 8; ++i) o += red[i][tid];
  po[(size_t)blockIdx.x * DH + tid] = o;
  if (tid == 0) { pm[blockIdx.x] = m; pl[blockIdx.x] = l; }
}

__global__ void __launch_bounds__(448) k_wo(const float* pm, const float* pl, const float* po, const u16* wo, float* yp) {
  __shared__ float os[B][64];
  const int tid = threadIdx.x, j = blockIdx.x, h = j / 4, d0 = (j % 4) * 64;
  if (tid < 128) {
    const int b = tid >> 6, d = d0 + (tid & 63), base = (b * H + h) * NSPLIT;
    float M = -INFINITY;
    for (int s = 0; s < NSPLIT; ++s) M = fmaxf(M, pm[base + s]);
    float L = 0.f, o = 0.f;
    for (int s = 0; s < NSPLIT; ++s) { const float wgt = __expf(pm[base + s] - M); L += wgt * pl[base + s]; o += wgt * po[(size_t)(base + s) * DH + d]; }
    os[b][tid & 63] = rnd(o / L);
  }
  __syncthreads();
  float a0 = 0.f, a1 = 0.f;
#pragma unroll 16
  for (int k = 0; k < 64; ++k) { const float wv = bf2f(wo[(size_t)(j * 64 + k) * D + tid]); a0 += os[0][k] * wv; a1 += os[1][k] * wv; }
  yp[(j * B + 0) * D + tid] = a0; yp[(j * B + 1) * D + tid] = a1;
}

__global__ void __launch_bounds__(512) k_gu(const u16* x, const float* yp, const u16* wg, const u16* wu, float* hbuf, float* act) {
  __shared__ float hs[B][D], hn[B][D], red[8][4][64];
  const int tid = threadIdx.x, lane = tid & 63, w = tid >> 6;
  for (int i = tid; i < B * D; i += 512) {
    float s = 0.f;
    for (int j = 0; j < 64; ++j) s += yp[j * B * D + i];
    const float hv = rnd(bf2f(x[i]) + rnd(s));
    hs[i / D][i % D] = hv;
    if (blockIdx.x == 0) hbuf[i] = hv;
  }
  __syncthreads();
  if (w < B) layernorm_to_lds(hs[w], hn[w], lane);
  __syncthreads();
  const int col = blockIdx.x * 64 + lane;
  float g0 = 0.f, g1 = 0.f, u0 = 0.f, u1 = 0.f;
#pragma unroll 8
  for (int k = w; k < D; k += 8) {
    const float a = bf2f(wg[(size_t)k * F + col]), b = bf2f(wu[(size_t)k * F + col]);
    g0 += hn[0][k] * a; g1 += hn[1][k] * a; u0 += hn[0][k] * b; u1 += hn[1][k] * b;
  }
  red[w][0][lane] = g0; red[w][1][lane] = g1; red[w][2][lane] = u0; red[w][3][lane] = u1;
  __syncthreads();
  if (w < B) {
    float g = 0.f, u = 0.f;
    for (int i = 0; i < 8; ++i) { g += red[i][w][lane]; u += red[i][2 + w][lane]; }
    act[w * F + col] = rnd(rnd(g) * rnd(u));
  }
}

__global__ void __launch_bounds__(1024) k_down(const float* hbuf, const float* act, const u16* wd, u16* out) {
  __shared__ float as[B][F], red[16][B][64];
  const int tid = threadIdx.x, lane = tid & 63, w = tid >> 6;
  for (int i = tid; i < B * F; i += 1024) as[i / F][i % F] = act[i];
  __syncthreads();
  const int col = blockIdx.x * 64 + lane;
  float a0 = 0.f, a1 = 0.f;
#pragma unroll 8
  for (int k = w; k < F; k += 16) { const float wv = bf2f(wd[(size_t)k * D + col]); a0 += as[0][k] * wv; a1 += as[1][k] * wv; }
  red[w][0][lane] = a0; red[w][1][lane] = a1;
  __syncthreads();
  if (w < B) {
    float s = 0.f;
    for (int i = 0; i < 16; ++i) s += red[i][w][lane];
    out[w * D + col] = f2bf(hbuf[w * D + col] + rnd(s));
  }
}

extern "C" void kernel_launch(void* const* d_in, const int* in_sizes, int n_in, void* d_out, int out_size, void* d_ws, size_t ws_size, hipStream_t stream) {
  const u16* x = (const u16*)d_in[0]; const u16* K = (const u16*)d_in[1]; const u16* V = (const u16*)d_in[2];
  const u16* wq = (const u16*)d_in[3]; const u16* wo = (const u16*)d_in[4];
  const u16* wg = (const u16*)d_in[6]; const u16* wu = (const u16*)d_in[7]; const u16* wd = (const u16*)d_in[8];
  float* ws = (float*)d_ws;
  float* q = ws; float* pm = q + B * HD; float* pl = pm + B * H * NSPLIT; float* po = pl + B * H * NSPLIT;
  float* yp = po + B * H * NSPLIT * DH; float* hbuf = yp + 64 * B * D; float* act = hbuf + B * D;
  k_q<<<HD / 64, 256, 0, stream>>>(x, wq, q);
  k_attn<<<B * H * NSPLIT, 256, 0, stream>>>(q, K, V, pm, pl, po);
  k_wo<<<64, 448, 0, stream>>>(pm, pl, po, wo, yp);
  k_gu<<<F / 64, 512, 0, stream>>>(x, yp, wg, wu, hbuf, act);
  k_down<<<D / 64, 1024, 0, stream>>>(hbuf, act, wd, (u16*)d_out);
}
```

```cpp
#include <hip/hip_runtime.h>
#include <stdint.h>

typedef unsigned short u16;
typedef unsigned u32;
typedef unsigned long long u64;
typedef u32 u32x2 __attribute__((ext_vector_type(2)));
typedef u32 u32x4 __attribute__((ext_vector_type(4)));
typedef float f32x4 __attribute__((ext_vector_type(4)));
typedef float f32x2 __attribute__((ext_vector_type(2)));
typedef __bf16 bf16x2 __attribute__((ext_vector_type(2)));
typedef __bf16 bf16x8 __attribute__((ext_vector_type(8)));
typedef __amdgpu_buffer_rsrc_t rsrc_t;
#define LAS __attribute__((address_space(3)))
#define DEV __device__ __forceinline__

constexpr int D = 448, HD = 4096, F = 1792;
constexpr int NBLK = 256, NTHR = 576;
constexpr int NROLE = 56;
constexpr u32 REGION = 1024 * 1024, GROUP = 128 * 1024;
constexpr int AUX_NT = 2, AUX_SC1 = 16;
constexpr u32 SENT = 0xFFFFFFFFu, OOB = 0x7FFFFFF0u;
constexpr float SCL = 0.0625f * 1.4426950408889634f;
constexpr int SPIN_G = 1 << 21, SPIN_L = 1 << 24;

constexpr int PACE = 218;
constexpr int T_OFF = 71, T_W = 82, T_V = 10, T_X = 6;
constexpr int LO = 3, W1 = 4, W2 = 5, HI = 8;
constexpr int TH1 = 30, TH2 = 150;
constexpr int TH2P = 80, TH2R = 60, TH2E = 60, TH2F = -25;
constexpr int RS = 6, VS = 6;
constexpr int LAG = 105, GAIN = 5;
constexpr int INFL0 = 4, STATIC = 12;
constexpr int FK = 8, FV = 8;
constexpr int THETA = 600;
constexpr int SDELAY = 2, HDELAY = 3, MDELAY = 4, WDELAY = 8, QDELAY = 8, XDELAY = 70, STDELAY = 50;
constexpr u32 QT = 280;
constexpr u32 CODE_KB = 24;

constexpr u32 W_EPOCH = 0, W_ALL = 4096, W_ST = 32768, W_MG = W_ST + 32768, W_SF = W_MG + 32768, W_QG = W_SF + 65536, W_SX = W_QG + 131072;
constexpr u32 W_OP = W_SX + NBLK * 2048, W_YP = W_OP + 8 * 16384, W_PP = W_YP + 16 * 16384, W_END = W_PP + 16 * 16384;
constexpr u32 CLR_PER = (W_END - W_OP) / NBLK, PROBE_BYTES = 16 * 8192;
static_assert(W_OP % 16384 == 0 && CLR_PER % 128 == 0 && W_END + PROBE_BYTES <= (2u << 20), "workspace layout");

constexpr int C_Q = 0, C_QR = 1, V_G = 2, C_O = 3, V_AL = 4, V_FW = 5, C_S = 6, C_P = 8, V_SLIP = 9, V_CLS = 10;
constexpr int L_QPART = 16, L_QS = L_QPART + 512, L_LNST = L_QS + 128, L_ORED = L_LNST + 16;
constexpr int L_RED = L_ORED + 1024, L_HS = L_RED + 1088, L_CS = L_HS + 2 * 256, L_AS = L_CS + 256, L_SC = L_AS + 4 * 32, L_P = L_SC + 2048, L_WORDS = L_P + 1024;

DEV float u2f(u32 v) { return __builtin_bit_cast(float, v); }
DEV u32 f2u(float v) { return __builtin_bit_cast(u32, v); }
DEV float blo(u32 v) { return u2f(v << 16); }
DEV float bhi(u32 v) { return u2f(v & 0xffff0000u); }
DEV u32 pk(float lo, float hi) { bf16x2 r = {(__bf16)lo, (__bf16)hi}; return __builtin_bit_cast(u32, r); }
DEV float rnd(float v) { return (float)(__bf16)v; }
DEV float dot2(u32 a, u32 b, float c) { return __builtin_amdgcn_fdot2_f32_bf16(__builtin_bit_cast(bf16x2, a), __builtin_bit_cast(bf16x2, b), c, false); }
template <int CTRL> DEV float dpp(float x) { return u2f(__builtin_amdgcn_mov_dpp((int)f2u(x), CTRL, 0xf, 0xf, true)); }
constexpr int XOR1 = 0xB1, XOR2 = 0x4E, XOR7 = 0x141, XOR8 = 0x128, ROR4 = 0x124, BCAST = 0x150;
template <int CTRL> DEV int lane_perm(int x) { int r = __builtin_amdgcn_mov_dpp(x, CTRL, 0xf, 0xf, true); asm("" : "+v"(r)); return r; }
DEV float x16_sum(float x) { auto s = __builtin_amdgcn_permlane16_swap(f2u(x), f2u(x), false, false); return u2f(s[0]) + u2f(s[1]); }
DEV float x32_sum(float x) { auto s = __builtin_amdgcn_permlane32_swap(f2u(x), f2u(x), false, false); return u2f(s[0]) + u2f(s[1]); }
DEV float fold32(float a, float b) { auto s = __builtin_amdgcn_permlane32_swap(f2u(a), f2u(b), false, false); return u2f(s[0]) + u2f(s[1]); }
DEV float x16_max(float x) { auto s = __builtin_amdgcn_permlane16_swap(f2u(x), f2u(x), false, false); return fmaxf(u2f(s[0]), u2f(s[1])); }
DEV float x32_max(float x) { auto s = __builtin_amdgcn_permlane32_swap(f2u(x), f2u(x), false, false); return fmaxf(u2f(s[0]), u2f(s[1])); }
DEV float row_sum(float x) { x += dpp<XOR1>(x); x += dpp<XOR2>(x); x += dpp<XOR7>(x); x += dpp<XOR8>(x); return x; }
DEV float row_max(float x) { x = fmaxf(x, dpp<XOR1>(x)); x = fmaxf(x, dpp<XOR2>(x)); x = fmaxf(x, dpp<XOR7>(x)); x = fmaxf(x, dpp<XOR8>(x)); return x; }
DEV float half_sum(float x) { return x16_sum(row_sum(x)); }
DEV u32 perm_lo(u32 r0, u32 r1) { return __builtin_amdgcn_perm(r1, r0, 0x05040100u); }
DEV u32 perm_hi(u32 r0, u32 r1) { return __builtin_amdgcn_perm(r1, r0, 0x07060302u); }
DEV rsrc_t mkrsrc(const void* p, u32 bytes) { return __builtin_amdgcn_make_buffer_rsrc((void*)p, 0, bytes, 0x00020000); }
DEV f32x4 mfma(u32x4 a, u32x4 b, f32x4 c) { return __builtin_amdgcn_mfma_f32_16x16x32_bf16(__builtin_bit_cast(bf16x8, a), __builtin_bit_cast(bf16x8, b), c, 0, 0, 0); }
DEV u32 safe(u32 v) { return v < 0xFFFFFFFEu ? v : 0xFFFFFFFEu; }
DEV u32 umax4(u32x4 v) { return max(max(v[0], v[1]), max(v[2], v[3])); }
DEV bool none(bool c) { return __builtin_amdgcn_ballot_w64(c) == 0; }
template <class T> DEV T pin(T v) { asm("" : "+v"(v)); return v; }
DEV f32x4 sum4(const LAS float* p, int stride) { return pin(pin(*(const LAS f32x4*)p + *(const LAS f32x4*)(p + stride)) + pin(*(const LAS f32x4*)(p + 2 * stride) + *(const LAS f32x4*)(p + 3 * stride))); }
DEV float hsum(f32x4 v) { const f32x2 h = pin(f32x2{v[0], v[1]} + f32x2{v[2], v[3]}); return pin(h[0] + h[1]); }
#define CBAR() asm volatile("" ::: "memory")
#define SBAR() __builtin_amdgcn_sched_barrier(0)
#define WAIT_VM(n) asm volatile("s_waitcnt vmcnt(%0)" :: "n"(n) : "memory")
#define THROTTLE(el, el2, c) asm volatile("s_waitcnt vmcnt(%7)\n\ts_cmpk_ge_i32 %1, %5\n\ts_cbranch_scc1 1f\n\ts_waitcnt vmcnt(%2)\n\ts_cmpk_ge_i32 %0, %3\n\ts_cbranch_scc1 1f\n\t" \
    "s_waitcnt vmcnt(%4)\n\ts_cmpk_ge_i32 %0, %5\n\ts_cbranch_scc1 1f\n\ts_waitcnt vmcnt(%6)\n1:" \
    :: "s"(el), "s"(el2), "n"(W2 - 1), "n"((c) + TH1), "n"(W1 - 1), "n"(c), "n"(LO - 1), "n"(HI - 1) : "memory", "scc")
#define TAIL_BARRIER() asm volatile("s_waitcnt lgkmcnt(0)\n\ts_barrier" ::: "memory")
#define NOW() ((u32)__builtin_amdgcn_s_memrealtime())

DEV u32 lds_arrive(LAS u32* c) {
  u32 old; u64 save;
  asm volatile("s_mov_b64 %1, exec\n\ts_mov_b64 exec, 1\n\tds_add_rtn_u32 %0, %2, %3\n\ts_mov_b64 exec, %1\n\ts_waitcnt lgkmcnt(0)"
               : "=&v"(old), "=&s"(save) : "v"(c), "v"(1u) : "memory");
  return __builtin_amdgcn_readfirstlane(old);
}
DEV void lds_signal(LAS u32* c) {
  u64 save;
  asm volatile("s_mov_b64 %0, exec\n\ts_mov_b64 exec, 1\n\tds_add_u32 %1, %2\n\ts_mov_b64 exec, %0" : "=&s"(save) : "v"(c), "v"(1u) : "memory");
}
DEV u32 arrive_sum4(LAS u32* c, const LAS float* p, f32x4& sum) {
  u32 old; u64 save; f32x4 a, b, d, e;
  asm volatile("s_mov_b64 %5, exec\n\ts_mov_b64 exec, 1\n\tds_add_rtn_u32 %0, %6, %7\n\ts_mov_b64 exec, %5\n\t"
               "ds_read_b128 %1, %8\n\tds_read_b128 %2, %8 offset:1024\n\tds_read_b128 %3, %8 offset:2048\n\tds_read_b128 %4, %8 offset:3072\n\ts_waitcnt lgkmcnt(0)"
               : "=&v"(old), "=&v"(a), "=&v"(b), "=&v"(d), "=&v"(e), "=&s"(save) : "v"(c), "v"(1u), "v"(p) : "memory");
  sum = pin(pin(a + b) + pin(d + e));
  return __builtin_amdgcn_readfirstlane(old);
}
template <int SLEEP> DEV void lds_wait(LAS u32* c, u32 target) {
#pragma unroll 1
  for (int i = 0; i < SPIN_L; ++i) {
    const u32 v = __builtin_amdgcn_readfirstlane(*(volatile LAS u32*)c);
    if (v >= target) break;
    if (SLEEP) __builtin_amdgcn_s_sleep(SLEEP);
  }
  CBAR();
}
template <int N, u32 S2, u32 S1> DEV void gather(rsrc_t r, u32 off, u32 off_last, u32x4 (&v)[N]) {
  CBAR();
#pragma unroll
  for (int k = 0; k < N; ++k) v[k] = __builtin_amdgcn_raw_buffer_load_b128(r, k == N - 1 ? off_last : off, (k >> 1) * S2 + (k & 1) * S1, AUX_SC1);
}
template <int N> DEV u32 umax_all(const u32x4 (&v)[N]) {
  u32 m = 0;
#pragma unroll
  for (int k = 0; k < N; ++k) m = max(m, umax4(v[k]));
  return m;
}
template <int N, u32 S2, u32 S1> DEV void poll_sent(rsrc_t r, u32 off, u32x4 (&v)[N]) {
#pragma unroll 1
  for (int i = 0; i < SPIN_G; ++i) {
    gather<N, S2, S1>(r, off, off, v);
    if (umax_all(v) != SENT) break;
  }
  CBAR();
}
template <int N, u32 S2, u32 S1> DEV f32x4 poll_sum(rsrc_t r, u32 off, u32 off_last) {
  f32x4 s;
  int pass = 0;
  u64 again;
#pragma unroll 1
  do {
    u32x4 v[N]; f32x4 t[N / 2];
    gather<N, S2, S1>(r, off, off_last, v);
    again = ~0ull;
    if (__builtin_expect((++pass & 63) == 0, 0)) again = pass < SPIN_G ? __builtin_amdgcn_ballot_w64(umax_all(v) == SENT) : 0;
#pragma unroll
    for (int k = 0; k < N / 2; ++k) t[k] = __builtin_bit_cast(f32x4, v[2 * k]) + __builtin_bit_cast(f32x4, v[2 * k + 1]);
    s = N == 8 ? (t[0] + t[1]) + (t[N / 2 - 2] + t[N / 2 - 1]) : t[0] + t[1];
    again &= __builtin_amdgcn_ballot_w64(__builtin_isunordered(s[0], s[1])) | __builtin_amdgcn_ballot_w64(__builtin_isunordered(s[2], s[3]));
  } while (again != 0);
  CBAR();
  return s;
}
template <int HALF, int CTRL> DEV void tr_pair(float (&x)[16], int h, int c) {
  const float t1 = x[h] + dpp<CTRL>(x[h]), t2 = x[h + HALF] + dpp<CTRL>(x[h + HALF]);
  x[h] = (c & HALF) ? t2 : t1;
}
DEV void tr_step(float (&x)[16], int j, int c) {
  if (j >= 8) tr_pair<8, XOR8>(x, j - 8, c);
  if (j >= 12) tr_pair<4, XOR7>(x, j - 12, c);
  if (j >= 14) tr_pair<2, XOR2>(x, j - 14, c);
  if (j == 15) tr_pair<1, XOR1>(x, 0, c);
}
DEV float dot8(u32x4 k, u32x4 q) { return dot2(k[3], q[3], dot2(k[2], q[2], dot2(k[1], q[1], dot2(k[0], q[0], 0.f)))); }
template <int J> DEV void pv_step(f32x2 (&acc)[4], u32x4 v, float prow) {
  const float p = dpp<BCAST + J>(prow);
  const f32x2 p2 = {p, p};
#pragma unroll
  for (int e = 0; e < 4; ++e) { const f32x2 v2 = {blo(v[e]), bhi(v[e])}; acc[e] = __builtin_elementwise_fma(p2, v2, acc[e]); }
  asm volatile("" : "+v"(acc[0]), "+v"(acc[1]), "+v"(acc[2]), "+v"(acc[3]));
}
DEV float ln_inv(float var) { return __builtin_amdgcn_rcpf(rnd(__builtin_amdgcn_sqrtf(rnd(rnd(var) + 1e-5f)))); }

DEV float ln_scale(u32x4 xa, u32x4 xb, float mean_a, float mean_b, int lane) {
  f32x2 qa = {0.f, 0.f}, qb = {0.f, 0.f};
#pragma unroll
  for (int e = 0; e < 4; ++e) {
    const f32x2 da = f32x2{blo(xa[e]), bhi(xa[e])} - mean_a, db = f32x2{blo(xb[e]), bhi(xb[e])} - mean_b;
    qa = __builtin_elementwise_fma(da, da, qa); qb = __builtin_elementwise_fma(db, db, qb);
  }
  return ln_inv(half_sum(fold32(lane < 56 ? qa[0] + qa[1] : 0.f, lane < 56 ? qb[0] + qb[1] : 0.f)) * (1.f / D));
}
DEV u32 near_class(unsigned char* p, int lane) {
  __builtin_amdgcn_raw_buffer_store_b32(0, mkrsrc(p, PROBE_BYTES), lane * (8192 + 384), 0, AUX_SC1);
  WAIT_VM(0);
  u32 best0 = ~0u, best1 = ~0u;
#pragma unroll 1
  for (int i = 0; i < 16; ++i) {
    const unsigned char* a = p + i * (8192 + 384);
    u64 t0, t1; u32 v;
    asm volatile("s_memtime %0\n\ts_load_dword %2, %3, 0x0\n\ts_waitcnt lgkmcnt(0)\n\ts_memtime %1\n\ts_waitcnt lgkmcnt(0)" : "=&s"(t0), "=&s"(t1), "=&s"(v) : "s"(a) : "memory");
    const u32 d = (u32)(t1 - t0);
    if (i & 1) best1 = min(best1, d); else best0 = min(best0, d);
  }
  return best1 < best0;
}
DEV u32 touch_code(u64 text, int bh, u32 lane) {
  return *(const __attribute__((address_space(1))) u32*)((const __attribute__((address_space(1))) char*)(text & ~255ull) + (min(lane, CODE_KB / 2 - 1u) + bh * (CODE_KB / 2u)) * 64u);
}
DEV void clear_slots(rsrc_t rw, int blk, int lane) {
  const u32x4 s4 = {SENT, SENT, SENT, SENT};
#pragma unroll
  for (u32 k = 0; k < CLR_PER; k += 1024) {
    const u32 o = W_OP + blk * CLR_PER + k + lane * 16;
    const u32 m = o >= W_PP ? 0x1c00u : o >= W_YP ? 0xe00u : 0x80000000u, skip = k + lane * 16 < CLR_PER ? m : 0u;
    __builtin_amdgcn_raw_buffer_store_b128(s4, rw, (o & skip) == skip ? OOB : o, 0, AUX_SC1);
  }
}
DEV void to_frags(const u32x2 (&raw)[8], u32x4 (&f)[4]) {
#pragma unroll
  for (int i = 0; i < 4; ++i) {
    f[0][i] = perm_lo(raw[2 * i][0], raw[2 * i + 1][0]); f[1][i] = perm_hi(raw[2 * i][0], raw[2 * i + 1][0]);
    f[2][i] = perm_lo(raw[2 * i][1], raw[2 * i + 1][1]); f[3][i] = perm_hi(raw[2 * i][1], raw[2 * i + 1][1]);
  }
}
typedef const __attribute__((address_space(1))) u16* gptr;
DEV void load_tile(u32x4 (&wf)[4][4], gptr base, u32 pitch, bool full) {
  u32x2 raw[4][8];
#define ROW(t, j) { raw[t][j] = __builtin_nontemporal_load((const __attribute__((address_space(1))) u32x2*)(base + (32 * (t) + (j)) * pitch)); SBAR(); }
#pragma unroll
  for (int j = 0; j < 16; ++j) { raw[2 + (j >> 3)][j & 7] = u32x2{0, 0}; ROW(j >> 3, j & 7) }
  if (full) {
#pragma unroll
    for (int j = 16; j < 32; ++j) ROW(j >> 3, j & 7)
  }
#undef ROW
#pragma unroll
  for (int t = 0; t < 4; ++t) {
    to_frags(raw[t], wf[t]);
    asm volatile("" : "+v"(wf[t][0]), "+v"(wf[t][1]), "+v"(wf[t][2]), "+v"(wf[t][3]));
    SBAR();
  }
}
DEV bool tag_ok(u32 lo, u32 hi, u32 tag_lo, u32 tag_hi) { return hi == tag_hi && (lo & 0xffu) == tag_lo; }

struct Args { const u16 *x, *K, *V, *wq, *wo, *wg, *wu, *wd; u16* out; unsigned char* ws; };
typedef const __attribute__((address_space(4))) Args* args_ptr;

__global__ void __launch_bounds__(NTHR, 3)
fused_decode(Args) {
  __shared__ __attribute__((aligned(16))) u32 smem[L_WORDS];
  LAS u32* lds = (LAS u32*)smem;
  LAS float* ldf = (LAS float*)smem;
  const int tid = threadIdx.x, lane = tid & 63, wave = __builtin_amdgcn_readfirstlane(tid >> 6);
  const int blk = blockIdx.x, bh = blk >> 3, bb = bh >> 4, head = bh & 15, reg = blk & 3, side = (blk >> 2) & 1;
  args_ptr ap = (args_ptr)__builtin_amdgcn_kernarg_segment_ptr();
  u64 text; asm volatile("s_getpc_b64 %0" : "=s"(text));
  asm volatile("s_cmp_lg_u32 %1, 8\n\ts_cbranch_scc1 1f\n\tds_write_b32 %2, %3\n\ts_waitcnt lgkmcnt(0)\n1:\n\t"
               "s_load_dword s96, %0, 0x0\n\ts_load_dword s97, %0, 0x40\n\ts_barrier"
               : "+s"(ap) : "s"(wave), "v"(lds + (lane & 15)), "v"(0u) : "memory", "scc", "s96", "s97");
  u32 pf;
  asm volatile("" : "=v"(pf));
  if (wave == 8) pf = touch_code(text, bh, lane);
  unsigned char* ws = ap->ws;
  const int r = lane & 15, g = lane >> 4;
  const size_t reg_off = (size_t)(bh * 4 + reg) * REGION;

  if (wave >= 5) {
    const u16 *x = ap->x, *wq = ap->wq;
    asm volatile("s_setprio 3\n\ts_sleep %3" : "+s"(x), "+s"(wq), "+s"(ws) : "n"(HDELAY));
    const rsrc_t rw = mkrsrc(ws, W_END), rx = mkrsrc(x, 2 * D * 2);
    const int tw = wave - 5, hb = lane >> 5, f = lane & 31;
    const u32 t_help = NOW();
    const f32x4 zero = {0.f, 0.f, 0.f, 0.f};
    const int role = blk < NROLE ? 1 : blk < 2 * NROLE ? 2 : 0;
    const int rid = (blk & 7) * 7 + bh - 7;
    u32x2 ep = __builtin_amdgcn_raw_buffer_load_b64(rw, W_EPOCH, 0, AUX_SC1);
    u32x4 wf[4][4];
    if (blk >= 128) {
      const int cs = blk & 63, ks = (blk >> 6) & 1, row0 = 224 * ks + 32 * tw + 8 * g;
      const rsrc_t rq = mkrsrc(wq, D * HD * 2);
      const u32 xo = lane < 56 ? lane * 16 : OOB, xfo = ((r >> 2) & 1) * (D * 2) + row0 * 2, wo0 = row0 * (HD * 2) + cs * 128 + r * 8;
      const u32x4 xa = __builtin_amdgcn_raw_buffer_load_b128(rx, xo, 0, 0), xb = __builtin_amdgcn_raw_buffer_load_b128(rx, xo, D * 2, 0);
      u32x4 xf[2]; u32x2 raw[2][8];
#pragma unroll
      for (int t = 0; t < 2; ++t) xf[t] = __builtin_amdgcn_raw_buffer_load_b128(rx, t && tw == 3 ? OOB : xfo, t * 256, 0);
#pragma unroll
      for (int t = 0; t < 2; ++t)
#pragma unroll
        for (int j = 0; j < 8; ++j) raw[t][j] = __builtin_amdgcn_raw_buffer_load_b64(rq, t && tw == 3 ? OOB : wo0, (128 * t + j) * (HD * 2), AUX_NT);
      SBAR();
      float sa = 0.f, sb = 0.f;
#pragma unroll
      for (int e = 0; e < 4; ++e) { sa = dot2(xa[e], 0x3F803F80u, sa); sb = dot2(xb[e], 0x3F803F80u, sb); }
      const float mean = half_sum(fold32(sa, sb)) * (1.f / D);
      const auto mn = __builtin_amdgcn_permlane32_swap(f2u(mean), f2u(mean), false, false);
      const float inv = ln_scale(xa, xb, u2f(mn[0]), u2f(mn[1]), lane);
      const auto ii = __builtin_amdgcn_permlane32_swap(f2u(inv), f2u(inv), false, false);
      const float mu = rnd(u2f((lane & 4) ? mn[1] : mn[0])), iv = u2f((lane & 4) ? ii[1] : ii[0]);
      f32x4 d[4] = {zero, zero, zero, zero};
#pragma unroll
      for (int t = 0; t < 2; ++t) {
        u32x4 xn, fr[4];
#pragma unroll
        for (int e = 0; e < 4; ++e) { const u32 c = pin(pk(blo(xf[t][e]) - mu, bhi(xf[t][e]) - mu)); xn[e] = pk(blo(c) * iv, bhi(c) * iv); }
        to_frags(raw[t], fr);
#pragma unroll
        for (int n = 0; n < 4; ++n) d[n] = mfma(xn, fr[n], d[n]);
      }
      *(LAS f32x4*)(ldf + L_QPART + tw * 128 + f * 4) = f32x4{d[0][0], d[1][0], d[2][0], d[3][0]};
      if (lds_arrive(lds + C_Q) == 3) {
        const LAS f32x2* p = (const LAS f32x2*)(ldf + L_QPART + lane * 2);
        f32x2 t = (p[0] + p[64]) + (p[128] + p[192]);
        t += t * 1.07e-5f;
        const u64 e1 = ((u64)ep[1] << 32 | ep[0]) + 1;
        const u32 th = (u32)(e1 >> 8), tl = (u32)e1 & 0xffu;
        __builtin_amdgcn_raw_buffer_store_b128(u32x4{(f2u(t[0]) & 0xffffff00u) | tl, th, (f2u(t[1]) & 0xffffff00u) | tl, th}, rw, W_QG + ((hb * 64 + cs) * 2 + ks) * 512 + f * 16, 0, AUX_SC1);
      }
    }
    if (tw == 3) clear_slots(rw, blk, lane);
    if (role != 0) {
      const u16 *wa = role == 1 ? ap->wo : ap->wg, *wb = ap->wu;
      asm volatile("s_sleep %2" : "+s"(wa), "+s"(wb) : "n"(WDELAY));
      const gptr wbase = role == 1 ? (gptr)wa + (512 * (blk & 7) + 128 * tw + 8 * g) * D + bh * 64 + 4 * r
                                   : (gptr)(r < 8 ? wa : wb) + (128 * tw + 8 * g) * F + rid * 32 + 4 * (r & 7);
      load_tile(wf, wbase, role == 1 ? D : F, role == 1 || tw < 3);
      if (role == 2) {
        const u32x4 ones = {0x3F803F80u, 0x3F803F80u, 0x3F803F80u, 0x3F803F80u};
#pragma unroll
        for (int n = 0; n < 4; ++n) ldf[L_CS + (4 * r + n) * 4 + tw] = mfma(ones, wf[3][n], mfma(ones, wf[2][n], mfma(ones, wf[1][n], mfma(ones, wf[0][n], zero))))[0];
      }
    }
    asm volatile("" : "+v"(ep) : "v"(pf));
    const u64 epoch = ((u64)ep[1] << 32 | ep[0]) + 1;
    const u32 tag_hi = (u32)(epoch >> 8), tag_lo = (u32)epoch & 0xffu;
    {
      u32x4 a;
#pragma unroll 1
      for (int it = 0; it < 4096 && NOW() - t_help < QT; ++it) __builtin_amdgcn_s_sleep(1);
#pragma unroll 1
      for (int it = 0; it < SPIN_G; ++it) {
        CBAR();
        a = __builtin_amdgcn_raw_buffer_load_b128(rw, W_QG + (bh * 4 + tw) * 1024 + lane * 16, 0, AUX_SC1);
        if (none(!(tag_ok(a[0], a[1], tag_lo, tag_hi) && tag_ok(a[2], a[3], tag_lo, tag_hi)))) break;
        __builtin_amdgcn_s_sleep(QDELAY);
      }
      lds[L_QS + tw * 32 + f] = pk(x32_sum(u2f(a[0] & 0xffffff00u)), x32_sum(u2f(a[2] & 0xffffff00u)));
      WAIT_VM(0);
      lds_signal(lds + C_QR);
    }
    __builtin_amdgcn_s_setprio(0);

    if (role == 0) {
      if (blk == 127 && tw == 0) {
        const u32x2 fl = {(u32)epoch, (u32)(epoch >> 32)};
        lds_wait<16>(lds + C_S, 4);
        const u32 gcls = near_class(ws + W_END, lane) ^ side;
#pragma unroll 1
        for (int it = 0; it < SPIN_G; ++it) {
          __builtin_amdgcn_s_sleep(32);
          CBAR();
          u32 bad = 0;
#pragma unroll
          for (int k = 0; k < 4; ++k) { const u32x2 a = __builtin_amdgcn_raw_buffer_load_b64(rw, W_SF + (k * 64 + lane) * 128, 0, AUX_SC1); bad |= (a[0] ^ fl[0]) | (a[1] ^ fl[1]); }
          if (none(bad != 0)) break;
        }
        CBAR();
        __builtin_amdgcn_raw_buffer_store_b64(lane == 0 ? fl : u32x2{gcls << 8 | tag_lo, tag_hi}, rw, lane == 0 ? W_EPOCH : lane <= 8 ? W_ALL + (lane - 1) * 256 : OOB, 0, AUX_SC1);
      }
    } else if (role == 1) {
      lds_wait<8>(lds + C_P, 3);
      const int kp = blk & 7, np = bh;
      const u32 gx = *(volatile LAS u32*)(lds + V_G) << 13;
      const u32 yoff = (W_YP + (tw * 4 + (kp >> 1)) * 16384 + (g & 1) * 8192 + (kp & 1) * 4096 + ((g >> 1) * D + np * 64 + r * 4) * 4) ^ gx;
      const u32 aoff = (W_OP + ((kp & 3) * 2 + (tw >> 1)) * 16384 + (kp >> 2) * 8192 + (tw & 1) * 4096 + r * 128 + g * 16) ^ gx;
      asm volatile("" :: "v"(yoff), "v"(aoff));
      lds_wait<1>(lds + C_O, 4);
      u32x4 a[4];
      poll_sent<4, 2048, 64>(rw, aoff, a);
      f32x4 d[4] = {zero, zero, zero, zero};
#pragma unroll
      for (int t = 0; t < 4; ++t)
#pragma unroll
        for (int n = 0; n < 4; ++n) d[n] = mfma(a[t], wf[t][n], d[n]);
      f32x4 e;
#pragma unroll
      for (int n = 0; n < 4; ++n) e[n] = hsum(d[n]);
      *(LAS f32x4*)(ldf + L_RED + tw * 256 + lane * 4) = e;
      TAIL_BARRIER();
      const f32x4 t = sum4(ldf + L_RED + lane * 4, 256);
      f32x4 ya, yb;
#pragma unroll
      for (int n = 0; n < 4; ++n) { const auto w = __builtin_amdgcn_permlane16_swap(f2u(t[n]), f2u(t[n]), false, false); ya[n] = u2f(w[0]); yb[n] = u2f(w[1]); }
      ya = pin(ya) + pin(yb);
      const u32x4 y = {safe(f2u(ya[0])), safe(f2u(ya[1])), safe(f2u(ya[2])), safe(f2u(ya[3]))};
      __builtin_amdgcn_raw_buffer_store_b128(y, rw, yoff, 0, AUX_SC1);
    } else {
      const int col = 128 * tw + 4 * f;
      const u32x2 xres = __builtin_amdgcn_raw_buffer_load_b64(rx, col < D ? (hb * D + col) * 2 : OOB, 0, 0);
      const rsrc_t rd = mkrsrc(ap->wd, F * D * 2);
      const int jp = 8 * tw + (r >> 1);
      u32 doff = jp < 28 ? ((rid * 32 + 8 * g) * D + 128 * tw + 8 * r) * 2 : OOB;
      const int jo = (blk & 7) * 4 + bh - 7, ob = (lane >> 1) & 1, oc = 16 * jo + 8 * tw + (lane & 1) * 4 + hb * 2;
      const bool writer = bh < 11 && jo < 28 && tw < 2;
      const rsrc_t ro = mkrsrc(ap->out, 2 * D * 2);
      u32 ooff = writer && f < 4 ? (ob * D + oc) * 2 : OOB;
      asm volatile("" : "+v"(doff), "+v"(ooff));
      lds_wait<8>(lds + C_P, 3);
      __builtin_amdgcn_raw_buffer_store_b32(0, ro, ooff, 0, 0);
      { const u32 t = __builtin_amdgcn_raw_buffer_load_b32(rd, lane == 0 ? doff : OOB, 0, AUX_NT); asm volatile("" :: "v"(t)); }
      WAIT_VM(0);
      lds_wait<1>(lds + C_O, 4);
      u32x4 wdn[8];
      {
        u32x4 raw[8];
#pragma unroll
        for (int j = 0; j < 8; ++j) raw[j] = __builtin_amdgcn_raw_buffer_load_b128(rd, doff, j * (D * 2), AUX_NT);
#pragma unroll
        for (int e = 0; e < 4; ++e) {
#pragma unroll
          for (int i = 0; i < 4; ++i) { wdn[2 * e][i] = perm_lo(raw[2 * i][e], raw[2 * i + 1][e]); wdn[2 * e + 1][i] = perm_hi(raw[2 * i][e], raw[2 * i + 1][e]); }
          asm volatile("" : "+v"(wdn[2 * e]), "+v"(wdn[2 * e + 1]));
        }
      }
      const u32 gx = *(volatile LAS u32*)(lds + V_G) << 13;
      const u32 yoff = col < D ? (W_YP + (blk & 3) * 65536 + side * 8192 + (hb * D + col) * 4) ^ gx : OOB;
      u32 poff = jp < 28 ? (W_PP + (jp & 15) * 16384 + (jp >> 4) * 8192 + rid * 128 + (r & 1) * 64 + (g & 1) * 32 + (g >> 1) * 16) ^ gx : OOB;
      LAS u32* hw = lds + L_HS + hb * 256 + col / 2;
      LAS u32* hr = lds + L_HS + ((r >> 2) & 1) * 256 + 64 * tw + 4 * g;
      LAS float* sw = ldf + L_LNST + hb * 4 + tw;
      LAS float* rw4 = ldf + L_RED + lane * 4 + tw;
      LAS float* rg = ldf + L_RED + (f & 3) * 272 + hb * 64 + (f & 28);
      f32x2 cs = {hsum(*(LAS f32x4*)(ldf + L_CS + f * 4)), hsum(*(LAS f32x4*)(ldf + L_CS + 128 + f * 4))};
      LAS float* sr = ldf + L_LNST + hb * 4;
      LAS u16* aw = (LAS u16*)(lds + L_AS + tw * 32) + lane;
      LAS u32* ar = lds + L_AS + tw * 32 + ((r >> 2) & 1) * 16 + 4 * g;
      f32x2 x01 = {blo(xres[0]), bhi(xres[0])}, x23 = {blo(xres[1]), bhi(xres[1])};
      asm volatile("" : "+v"(hw), "+v"(hr), "+v"(sw), "+v"(rw4), "+v"(rg), "+v"(cs), "+v"(sr), "+v"(aw), "+v"(ar), "+v"(poff), "+v"(x01), "+v"(x23));
      const f32x4 y = poll_sum<8, 16384, 4096>(rw, yoff, yoff);
      const u32 y01 = pin(pk(y[0], y[1])), y23 = pin(pk(y[2], y[3]));
      const u32x2 hp = {pk(x01[0] + blo(y01), x01[1] + bhi(y01)), pk(x23[0] + blo(y23), x23[1] + bhi(y23))};
      *(LAS u32x2*)hw = hp;
      CBAR();
      u32x4 a[4];
#pragma unroll
      for (int t = 0; t < 4; ++t) a[t] = *(LAS u32x4*)(hr + 16 * t);
      SBAR();
      *sw = half_sum(dot2(hp[1], 0x3F803F80u, dot2(hp[0], 0x3F803F80u, 0.f)));
      SBAR();
      f32x4 d[4] = {zero, zero, zero, zero};
#pragma unroll
      for (int t = 0; t < 4; ++t)
#pragma unroll
        for (int n = 0; n < 4; ++n) d[n] = mfma(a[t], wf[t][n], d[n]);
      asm("" : "+v"(d[0]), "+v"(d[1]), "+v"(d[2]), "+v"(d[3]));
#pragma unroll
      for (int n = 0; n < 4; ++n) rw4[272 * n] = d[n][0];
      TAIL_BARRIER();
      const f32x4 sv = *(LAS f32x4*)sr, pg = *(LAS f32x4*)rg, pu = *(LAS f32x4*)(rg + 32);
      const float mb = rnd(hsum(sv) * (1.f / D));
      *aw = (u16)pk((hsum(pg) - mb * cs[0]) * (hsum(pu) - mb * cs[1]), 0.f);
      CBAR();
      const u32x4 aa = *(LAS u32x4*)ar;
      f32x4 dn[8];
#pragma unroll
      for (int n = 0; n < 8; ++n) dn[n] = mfma(aa, wdn[n], zero);
      asm("" : "+v"(dn[0]), "+v"(dn[1]), "+v"(dn[2]), "+v"(dn[3]), "+v"(dn[4]), "+v"(dn[5]), "+v"(dn[6]), "+v"(dn[7]));
      u32x4 pv;
#pragma unroll
      for (int n = 0; n < 4; ++n) { const float lo = dn[n][0], hi = dn[n + 4][0]; pv[n] = safe(f2u(hb ? hi : lo)); }
      __builtin_amdgcn_raw_buffer_store_b128(pv, rw, poff, 0, AUX_SC1);
      if (writer) {
        const u32 foff = (W_PP + (jo & 15) * 16384 + side * 8192 + (lane >> 2) * 128 + tw * 64 + (lane & 3) * 16) ^ gx;
        const u32 hq = lds[L_HS + ob * 256 + oc / 2];
        const float inv = ln_scale(*(LAS u32x4*)(lds + L_HS + lane * 4), *(LAS u32x4*)(lds + L_HS + 256 + lane * 4),
                                   hsum(*(LAS f32x4*)(ldf + L_LNST)) * (1.f / D), hsum(*(LAS f32x4*)(ldf + L_LNST + 4)) * (1.f / D), lane);
        const auto ii = __builtin_amdgcn_permlane32_swap(f2u(inv), f2u(inv), false, false);
        float inv2 = u2f(ob ? ii[1] : ii[0]);
        inv2 *= inv2;
        f32x2 h2 = {blo(hq), bhi(hq)};
        asm volatile("" : "+v"(inv2), "+v"(h2));
        f32x4 t = poll_sum<4, 4096, 2048>(rw, foff, lane < 32 ? foff : OOB);
        f32x2 u = {fold32(t[0], t[2]), fold32(t[1], t[3])};
#pragma unroll
        for (int e = 0; e < 2; ++e) { u[e] += dpp<ROR4>(u[e]); u[e] += dpp<XOR8>(u[e]); u[e] = x16_sum(u[e]) * inv2; }
        const u32 u01 = pin(pk(u[0], u[1]));
        __builtin_amdgcn_raw_buffer_store_b32(pk(h2[0] + blo(u01), h2[1] + bhi(u01)), ro, ooff, 0, 0);
      }
    }
    return;
  }

  const u16 *K = ap->K, *V = ap->V;
  int wave_first = wave;
  asm volatile("" : "+s"(wave_first));
  u32x2 fl; u32 cck, ccv;
  asm volatile("" : "=v"(fl), "=v"(cck), "=v"(ccv));
  if (__builtin_expect(wave_first == 0, 1)) {
    __builtin_amdgcn_s_setprio(3);
    const u32 seed = NOW();
    SBAR();
    const u32 po = (((seed + (blk >> 2)) << 7) & 0x3df80u) | 0x40000u;
    const u16* kv = (reg & 1) ? ap->V : ap->K;
    unsigned char* wsp = ws;
    asm volatile("; probe" : "+s"(kv), "+s"(wsp));
    const rsrc_t rk = mkrsrc((const unsigned char*)kv + reg_off, REGION), rw = mkrsrc(wsp, W_END);
    u64 t0, t1, t2, tr; u32 pa, pb; u32x2 ep;
    asm volatile("s_memtime %0\n\tbuffer_load_dword %4, off, %9, %7 nt\n\tbuffer_load_dword %5, off, %9, %8 nt\n\tbuffer_load_dwordx2 %6, off, %10, 0 sc1\n\ts_memrealtime %3\n\t"
                 "s_waitcnt vmcnt(2)\n\ts_memtime %1\n\ts_waitcnt vmcnt(1)\n\ts_memtime %2\n\ts_waitcnt vmcnt(0) lgkmcnt(0)"
                 : "=&s"(t0), "=&s"(t1), "=&s"(t2), "=&s"(tr), "=&v"(pa), "=&v"(pb), "=&v"(ep) : "s"(po), "s"(po | 8192), "s"(rk), "s"(rw) : "memory");
    const int mg = min((int)(u32)(t1 - t0), 1600) - 3 * min((int)(u32)(t2 - t1), 400);
    tr += min((u32)(t2 - t0), 8192u) * 21 >> 10;
    const u64 epoch = ((u64)ep[1] << 32 | ep[0]) + 1;
    fl = u32x2{(u32)epoch, (u32)(epoch >> 32)};
    const u32 die = (__builtin_amdgcn_s_getreg((20 << 0) | (0 << 6) | (3 << 11)) >> 2) & 1;
    __builtin_amdgcn_raw_buffer_store_b64(u32x2{(u32)mg << 19 | ((u32)tr & 1023) << 9 | die << 8 | (fl[0] & 0xffu), (u32)(epoch >> 8)}, rw, lane == 0 ? W_MG + blk * 128 : OOB, 0, AUX_SC1);
    const u32 tag_hi = (u32)(epoch >> 8), tag_lo = fl[0] & 0xffu;
    __builtin_amdgcn_s_sleep(MDELAY);
    u32x2 o;
#pragma unroll 1
    for (int it = 0; it < SPIN_G; ++it) {
      CBAR();
      o = __builtin_amdgcn_raw_buffer_load_b64(rw, W_MG + ((blk & ~15) + r) * 128, 0, AUX_SC1);
      if (none(!tag_ok(o[0], o[1], tag_lo, tag_hi))) break;
    }
    const int ow = lane_perm<ROR4>(o[0]), dv = (ow >> 19) - ((int)o[0] >> 19), dd = (ow & 256) ? -dv : dv;
    const int two = dd - lane_perm<XOR2>(dd), four = two + lane_perm<XOR8>(two), ev = abs(dd) >= THETA ? dd : four;
    const u32 mine = ((__builtin_amdgcn_readlane(o[0], blk & 11) >> 8) & 1) ^ side;
    cck = (__builtin_amdgcn_readlane(ev, (blk & 10) | 4) > 0) ^ mine; ccv = (__builtin_amdgcn_readlane(ev, (blk & 10) | 5) > 0) ^ mine;
    int dt = (int)(((o[0] >> 9) - (u32)tr) << 22) >> 22;
    dt += lane_perm<XOR1>(dt); dt += lane_perm<XOR2>(dt); dt += lane_perm<XOR7>(dt); dt += lane_perm<XOR8>(dt);
    const u32 mism = cck ^ ccv;
    if (bh >= 32 - FK) cck = ccv;
    if (bh < FV) ccv = cck;
    *(volatile LAS u32x2*)(lds + V_CLS) = u32x2{1 | cck << 1 | ccv << 2 | mism << 3, (u32)tr + (dt >> 4)};
  }

  if (wave == 0) {
    asm volatile("; control" : "+s"(ws));
    const rsrc_t rw = mkrsrc(ws, W_END);
    const u32 tag_hi = fl[1] << 24 | fl[0] >> 8, tag_lo = fl[0] & 0xffu;
    const u32 ck = __builtin_amdgcn_readfirstlane(cck), cv = __builtin_amdgcn_readfirstlane(ccv);
    float m = -INFINITY, l = 0.f;
    u32 t_k = 0;
#pragma unroll 1
    for (int hf = 0; hf < 2; ++hf) {
      lds_wait<3>(lds + C_S + hf, 4);
      t_k = NOW();
      { const u32 t = __builtin_amdgcn_raw_buffer_load_b32(mkrsrc((const unsigned char*)V + reg_off, REGION), hf == 0 && lane == 0 ? cv * 8192 : OOB, 0, AUX_NT); asm volatile("" :: "v"(t)); }
      if (hf == 0 || ck != cv) {
        const bool need = ck != cv || lane < 16;
        const u32x4 a = *(LAS u32x4*)(lds + L_SC + hf * 512 + lane * 8), b = *(LAS u32x4*)(lds + L_SC + hf * 512 + lane * 8 + 4);
        __builtin_amdgcn_raw_buffer_store_b128(u32x4{perm_hi(a[0], a[1]), perm_hi(a[2], a[3]), perm_hi(b[0], b[1]), perm_hi(b[2], b[3])}, rw,
                                               need ? W_SX + (hf * 256 + blk) * 1024 + lane * 16 : OOB, 0, AUX_SC1);
        WAIT_VM(0);
        __builtin_amdgcn_raw_buffer_store_b64(fl, rw, lane == 0 ? W_SF + (hf * 256 + blk) * 128 : OOB, 0, AUX_SC1);
        __builtin_amdgcn_s_sleep(XDELAY);
#pragma unroll 1
        for (int it = 0; it < SPIN_G; ++it) {
          __builtin_amdgcn_s_sleep(24);
          CBAR();
          const u32x2 f = __builtin_amdgcn_raw_buffer_load_b64(rw, W_SF + (hf * 256 + (blk ^ 4)) * 128, 0, AUX_SC1);
          if (none(f[0] != fl[0] || f[1] != fl[1])) break;
        }
        CBAR();
        const u32x4 p = __builtin_amdgcn_raw_buffer_load_b128(rw, need ? W_SX + (hf * 256 + (blk ^ 4)) * 1024 + lane * 16 : OOB, 0, AUX_SC1);
        *(LAS u32x4*)(lds + L_SC + 1024 + hf * 512 + lane * 8) = u32x4{p[0] << 16, p[0] & 0xffff0000u, p[1] << 16, p[1] & 0xffff0000u};
        *(LAS u32x4*)(lds + L_SC + 1024 + hf * 512 + lane * 8 + 4) = u32x4{p[2] << 16, p[2] & 0xffff0000u, p[3] << 16, p[3] & 0xffff0000u};
      }
      CBAR();
      float sc[8], mx = m;
#pragma unroll
      for (int e = 0; e < 8; ++e) {
        const int c = (lane & 1) * 8 + e, pg = 4 * (c >> 1) + 2 * cv + (c & 1);
        const int fixed = (lane >> 1) * 16 + (pg < STATIC ? pg + side * 1024 : pg - STATIC + (side ^ 1) * 1024);
        sc[e] = ldf[L_SC + (hf == 0 && lane < 16 && c < STATIC ? fixed : (ck == cv ? 0 : 1024) + hf * 512 + lane * 8 + e)];
        mx = fmaxf(mx, sc[e]);
      }
      mx = x32_max(x16_max(row_max(mx)));
      const float ms = mx == -INFINITY ? 0.f : mx, al = __builtin_amdgcn_exp2f((m - ms) * SCL);
      m = mx; l *= al;
#pragma unroll
      for (int e = 0; e < 8; ++e) { sc[e] = __builtin_amdgcn_exp2f((sc[e] - ms) * SCL); l += sc[e]; }
      *(LAS f32x4*)(ldf + L_P + hf * 512 + lane * 8) = f32x4{sc[0], sc[1], sc[2], sc[3]};
      *(LAS f32x4*)(ldf + L_P + hf * 512 + lane * 8 + 4) = f32x4{sc[4], sc[5], sc[6], sc[7]};
      ldf[V_AL] = al;
      lds_signal(lds + C_P);
    }
    const float mb = m, lb = x32_sum(half_sum(l));
    const int late = min(max((int)(t_k - lds[V_CLS + 1]) - (T_OFF + T_W + 135 * PACE / 16), 0) >> 1, 255);
    __builtin_amdgcn_raw_buffer_store_b128(u32x4{(f2u(mb) & 0xffff0000u) | late << 8 | tag_lo, tag_hi, (f2u(lb) & 0xffffff00u) | tag_lo, tag_hi}, rw,
                                           lane == 0 ? W_ST + (bh * 8 + reg * 2 + cv) * 128 : OOB, 0, AUX_SC1);
    u32x4 sb; u32x2 a;
    __builtin_amdgcn_s_sleep(STDELAY);
#pragma unroll 1
    for (int it = 0; it < SPIN_G; ++it) {
      __builtin_amdgcn_s_sleep(24);
      CBAR();
      sb = __builtin_amdgcn_raw_buffer_load_b128(rw, W_ST + (bh * 8 + (lane & 7)) * 128, 0, AUX_SC1);
      a = __builtin_amdgcn_raw_buffer_load_b64(rw, W_ALL + (blk & 7) * 256, 0, AUX_SC1);
      if (none(!(tag_ok(sb[0], sb[1], tag_lo, tag_hi) && tag_ok(sb[2], sb[3], tag_lo, tag_hi) && tag_ok(a[0], a[1], tag_lo, tag_hi)))) break;
    }
    CBAR();
    int lt = (sb[0] >> 8) & 255;
    lt += lane_perm<XOR1>(lt); lt += lane_perm<XOR2>(lt); lt += lane_perm<XOR7>(lt);
    lds[V_SLIP] = max((lt >> 2) - LAG, 0) * GAIN / 4;
    const float mi = u2f(sb[0] & 0xffff0000u);
    float M = fmaxf(mi, dpp<XOR1>(mi)); M = fmaxf(M, dpp<XOR2>(M)); M = fmaxf(M, dpp<XOR7>(M));
    float L = u2f(sb[2] & 0xffffff00u) * __builtin_amdgcn_exp2f((mi - M) * SCL); L += dpp<XOR1>(L); L += dpp<XOR2>(L); L += dpp<XOR7>(L);
    ldf[V_FW] = __builtin_amdgcn_exp2f((mb - M) * SCL) * __builtin_amdgcn_rcpf(L);
    lds[V_G] = (a[0] >> 8) & 1;
    lds_signal(lds + C_P);
    return;
  }

  {
    asm volatile("; stream" : "+s"(K), "+s"(V), "+s"(ws));
    const rsrc_t rw = mkrsrc(ws, W_END);
    const int sw = wave - 1;
    const unsigned char* kreg = (const unsigned char*)K + reg_off;
    const unsigned char* vreg = (const unsigned char*)V + reg_off;
    const rsrc_t rk = mkrsrc(kreg, REGION), rv = mkrsrc(vreg, REGION);
    __builtin_amdgcn_s_sleep(SDELAY);
    const u32 voff = (tid - 64) * 16;
#define LD(rs, vo, so) __builtin_amdgcn_raw_buffer_load_b128(rs, vo, so, AUX_NT)
#define STEP_OFF(j) ((u32)(((j) >> 1) * 16384 + ((j) & 1) * 4096))
#define PACE_STEP(j) \
    if ((j) % 2 == 0) el = __builtin_amdgcn_readfirstlane((int)(now - base)); \
    THROTTLE(el, el - th2, ((((j) & ~1) + 8) * PACE) >> 4);
    u32x4 bk[32], ring[16];
    u32 cv = 0, vk = voff + side * (STATIC * 4096), base = 0, now = 0;
    int th2 = blk < 2 * NROLE ? TH2R : TH2P, thk = TH2, mism = 0;
#pragma unroll
    for (int j = 0; j < 32; ++j) {
      if (j == STATIC) {
        u32 cls = 0;
#pragma unroll 1
        for (int i = 0; i < SPIN_L && !(cls & 1); ++i) {
          const u32x2 v = *(volatile LAS u32x2*)(lds + V_CLS);
          now = NOW();
          cls = __builtin_amdgcn_readfirstlane(v[0]); base = __builtin_amdgcn_readfirstlane(v[1]) + T_OFF;
        }
        cv = (cls >> 2) & 1; mism = cls >> 3; vk = voff + (cls & 2) * 4096;
        if (mism && bh >= 32 - FK) thk = th2 = TH2F;
      }
      if (j >= STATIC) THROTTLE((int)(now - base), (int)(now - base) - th2, (j * PACE) >> 4);
      else if (j >= INFL0) WAIT_VM(INFL0 - 1);
      bk[j] = LD(rk, vk, j < STATIC ? j * 4096u : STEP_OFF(j));
      if (j >= STATIC) now = NOW();
      SBAR();
    }
    __builtin_amdgcn_s_setprio(1);
    u32x4 q;
#pragma unroll 1
    for (int i = 0; i < SPIN_L; ++i) {
      const u32 n = *(volatile LAS u32*)(lds + C_QR);
      q = *(volatile LAS u32x4*)(lds + L_QS + 4 * (lane & 31));
      now = NOW();
      if (__builtin_amdgcn_readfirstlane(n) >= 4) break;
    }
    LAS float* scw = ldf + L_SC + sw * 32 + (lane >> 5) * 16 + r;
    const u32 vv = voff + cv * 8192;
    {
      float xx[16];
#pragma unroll
      for (int e = 0; e < 32; ++e) {
        if (e % 4 == 0) { SBAR(); THROTTLE((int)(now - base), (int)(now - base) - th2, ((32 + e / 4) * PACE) >> 4);
                          ring[e / 4] = LD(rk, vk, 2 * GROUP + STEP_OFF(e / 4)); now = NOW(); SBAR(); }
        xx[e & 15] = dot8(bk[e], q);
        tr_step(xx, e & 15, r);
        if ((e & 15) == 15) scw[(e >> 4) * 128] = rnd(x16_sum(xx[0]));
      }
    }
    base += 2 * PACE + T_W;
    th2 = blk < 2 * NROLE ? TH2R : thk;
    const u32 tx = mism ? T_X : 0;
    float pn = 0.f;
#pragma unroll 1
    for (int it = 2; it < 8; ++it) {
      float xx[16];
      const bool lastk = it == 7;
      if (it == RS) th2 = thk;
      if (lastk) { lds_wait<1>(lds + C_P, 1); pn = scw[L_P - L_SC]; }
      const rsrc_t r2 = mkrsrc(lastk ? vreg : kreg, REGION);
      const u32 b1 = it * GROUP, b2 = lastk ? 0 : b1 + GROUP, v2 = lastk ? vv : vk;
      int el = 0;
#pragma unroll
      for (int j = 0; j < 16; ++j) {
        PACE_STEP(j)
        ring[(j + 8) & 15] = j < 8 ? LD(rk, vk, b1 + STEP_OFF(j + 8)) : LD(r2, v2, b2 + STEP_OFF(j - 8));
        if (j % 2) now = NOW();
        SBAR(); xx[j] = dot8(ring[j], q); tr_step(xx, j, r); SBAR();
      }
      base += PACE;
      scw[it * 128] = rnd(x16_sum(xx[0]));
      if ((it & 3) == 3) { lds_signal(lds + C_S + (it >> 2)); base += tx; }
    }
    base += T_V;
    u32 vbase = base;
    f32x2 acc[4];
#pragma unroll
    for (int e = 0; e < 4; ++e) acc[e] = f32x2{0.f, 0.f};
    LAS float* ow = ldf + L_ORED + sw * 256 + (lane & 31) * 8 + (lane >> 5) * 4;
    LAS float* orr = ldf + L_ORED + lane * 4;
    u32 ooff = OOB;
    asm volatile("" : "+v"(ow), "+v"(orr));
    const u32 oslot = W_OP + (head & 7) * 16384 + (head >> 3) * 8192 + g * 2048 + (bb * 8 + reg * 2 + cv) * 128 + r * 8;
    float p = pn, al = 1.f;
    u32 b1 = 0;
    int el = 0;
    th2 = mism && bh < FV ? TH2F : TH2;
#define PV(J) PACE_STEP(J) ring[((J) + 8) & 15] = LD(rv, vv, b1 + STEP_OFF((J) + 8)); if ((J) % 2) now = NOW(); SBAR(); pv_step<J>(acc, ring[J], p); SBAR();
    PV(0) PV(1) PV(2) PV(3) PV(4) PV(5) PV(6) PV(7)
#pragma unroll 1
    for (int it = 1; it < 8; ++it) {
      al = 1.f;
      if (it == 4 || it == 7) { lds_wait<1>(lds + C_P, 2 + (it & 1)); al = ldf[V_AL + (it & 1)]; ooff = oslot ^ (lds[V_G] << 13); }
      pn = scw[L_P - L_SC + it * 128];
      const u32 slip = *(volatile LAS u32*)(lds + V_SLIP);
      PV(8) PV(9) PV(10) PV(11) PV(12) PV(13) PV(14) PV(15)
#pragma unroll
      for (int e = 0; e < 4; ++e) acc[e] *= al;
      p = it == 7 ? pn * al : pn;
      vbase += PACE; base = vbase + __builtin_amdgcn_readfirstlane(slip); b1 += GROUP;
      if (it == VS) th2 = min(th2, TH2E);
      PV(0) PV(1) PV(2) PV(3) PV(4) PV(5) PV(6) PV(7)
    }
#undef PV
    asm volatile("" :: "s"(now));
#define PV(J) pv_step<J>(acc, ring[J], p); SBAR();
    PV(8) PV(9) PV(10) PV(11) PV(12) PV(13) PV(14) PV(15)
#undef PV
    f32x4 o4;
#pragma unroll
    for (int e = 0; e < 4; ++e) o4[e] = fold32(acc[e >> 1][e & 1], acc[2 + (e >> 1)][e & 1]);
    *(LAS f32x4*)ow = o4;
    f32x4 t;
    if (arrive_sum4(lds + C_O, orr, t) == 3) {
      __builtin_amdgcn_raw_buffer_store_b64(u32x2{safe(pk(t[0], t[1])), safe(pk(t[2], t[3]))}, rw, ooff, 0, AUX_SC1);
    }
  }
}

extern "C" void kernel_launch(void* const* d_in, const int* in_sizes, int n_in, void* d_out, int out_size, void* d_ws, size_t ws_size, hipStream_t stream) {
  const uintptr_t span = ((uintptr_t)d_ws + ws_size - (2u << 20)) & ~(uintptr_t)((2u << 20) - 1);
  const Args a = {(const u16*)d_in[0], (const u16*)d_in[1], (const u16*)d_in[2], (const u16*)d_in[3], (const u16*)d_in[4],
                  (const u16*)d_in[6], (const u16*)d_in[7], (const u16*)d_in[8], (u16*)d_out, (unsigned char*)span};
  fused_decode<<<NBLK, NTHR, 0, stream>>>(a);
}
```
